```python
import torch

def _softmax(x, axis=-1):
    return torch.softmax(x, dim=axis)
def _gelu(x):
    return torch.nn.functional.gelu(x, approximate="tanh")
def _silu(x):
    return torch.nn.functional.silu(x)
def _sigmoid(x):
    return torch.sigmoid(x)
def _layernorm(x, axis=-1, eps=1e-5):
    m = x.mean(dim=axis, keepdim=True)
    v = x.var(dim=axis, keepdim=True, unbiased=False)
    return (x - m) / torch.sqrt(v + eps)
def _scatter_add(tgt, idx, src, axis=0):
    return tgt.clone().index_add_(axis, idx, src)
def _segment_sum(src, seg, n):
    out = torch.zeros((n,) + src.shape[1:], dtype=src.dtype)
    return out.index_add_(0, seg, src)
def _pad(x, pw):
    flat = tuple(v for pair in reversed(pw) for v in pair)
    return torch.nn.functional.pad(x, flat)

kernel_name = 'standalone__softmax_full_bidir__sampling'

def setup_inputs():

    _w = (lambda a: (lambda t: t.to(torch.bfloat16) if t.is_floating_point() else t)(torch.as_tensor(a)))
    B, H, Sq, Skv, Dh = 8, 32, 1, 4096, 128
    return {
        "q": _w((lambda *s: torch.randn(*s, dtype=torch.bfloat16) * 0.8)(B, H, Sq, Dh)),
        "k": _w((lambda *s: torch.randn(*s, dtype=torch.bfloat16) * 0.8)(B, H, Skv, Dh)),
        "v": _w((lambda *s: torch.randn(*s, dtype=torch.bfloat16) * 0.05)(B, H, Skv, Dh)),
    }

def reference(q, k, v):
    scale = q.shape[-1] ** -0.5
    scores = torch.einsum('bhqd,bhkd->bhqk', q, k) * scale
    attn = _softmax(scores, axis=-1)
    return torch.einsum('bhqk,bhkd->bhqd', attn, v)
```

```cpp
#include <hip/hip_runtime.h>
#include <hip/hip_bf16.h>
#include <stdint.h>

typedef unsigned u32x4 __attribute__((ext_vector_type(4)));
typedef float f32x4 __attribute__((ext_vector_type(4)));
typedef float f32x2 __attribute__((ext_vector_type(2)));
typedef __bf16 bf16x2 __attribute__((ext_vector_type(2)));

constexpr int D = 128, SKV = 4096;
constexpr float C2 = 0.088388347648318440f * 1.4426950408889634f;

__device__ __forceinline__ float dot2(unsigned k, unsigned q, float acc) {
  return __builtin_amdgcn_fdot2_f32_bf16(__builtin_bit_cast(bf16x2, k), __builtin_bit_cast(bf16x2, q), acc, false);
}
template <int CTRL> __device__ __forceinline__ float dpp(float x) {
  return __builtin_bit_cast(float, __builtin_amdgcn_mov_dpp(__builtin_bit_cast(int, x), CTRL, 0xf, 0xf, true));
}
constexpr int XOR1 = 0xB1, XOR2 = 0x4E, XOR7 = 0x141, XOR8 = 0x128, ROR4 = 0x124, NEWBCAST = 0x150;
__device__ __forceinline__ float xrow_max(float x) {
  auto s = __builtin_amdgcn_permlane16_swap(__float_as_uint(x), __float_as_uint(x), false, false);
  x = fmaxf(__uint_as_float(s[0]), __uint_as_float(s[1]));
  auto t = __builtin_amdgcn_permlane32_swap(__float_as_uint(x), __float_as_uint(x), false, false);
  return fmaxf(__uint_as_float(t[0]), __uint_as_float(t[1]));
}
__device__ __forceinline__ float xrow_sum(float x) {
  auto s = __builtin_amdgcn_permlane16_swap(__float_as_uint(x), __float_as_uint(x), false, false);
  x = __uint_as_float(s[0]) + __uint_as_float(s[1]);
  auto t = __builtin_amdgcn_permlane32_swap(__float_as_uint(x), __float_as_uint(x), false, false);
  return __uint_as_float(t[0]) + __uint_as_float(t[1]);
}
template <int I> struct IC { static constexpr int v = I; };
template <int N, int I = 0, class F> __device__ __forceinline__ void sfor(F&& f) {
  if constexpr (I < N) { f(IC<I>{}); sfor<N, I + 1>(f); }
}

template <int NW, int W, int G, int AUX, int PD, int PDV, int RAMP>
__global__ __launch_bounds__(NW * 64, 1) void attn3_k(const void* __restrict__ q, const void* __restrict__ k, const void* __restrict__ v,
                                                  __hip_bfloat16* __restrict__ out) {
  constexpr int NL = SKV / 4 / NW;
  constexpr int NG = NL / 16;
  static_assert(16 % W == 0 && 16 % G == 0 && PD <= W && PDV <= W && PD <= PDV, "");
  __shared__ __attribute__((aligned(16))) float lds[NW * (D + 4)];
  __shared__ float sc[NW * NG * 64];
  const int head = blockIdx.x;
  const int lane = threadIdx.x & 63, wave = __builtin_amdgcn_readfirstlane(threadIdx.x >> 6), c = lane & 15;
  constexpr unsigned HB = SKV * D * 2;
  const size_t hb = (size_t)head * HB;
  auto rk = __builtin_amdgcn_make_buffer_rsrc((void*)((const char*)k + hb), 0, HB, 0x00020000);
  auto rv = __builtin_amdgcn_make_buffer_rsrc((void*)((const char*)v + hb), 0, HB, 0x00020000);
  const u32x4 qv = *reinterpret_cast<const u32x4*>((const char*)q + (size_t)head * (D * 2) + c * 16);
  const int voff = lane * 16 + wave * (G * 1024);
  constexpr unsigned GS = 16 * NW * 1024;
  u32x4 buf[W];
  auto ld = [&](auto rs, unsigned gb, auto jc) {
    constexpr int jj = decltype(jc)::v;
    constexpr int off = (jj / G) * NW * G * 1024 + (jj % G) * 1024;
    constexpr int so = off & ~4095, io = off & 4095;
    return __builtin_amdgcn_raw_buffer_load_b128(rs, voff + io, gb + so, AUX);
  };
  sfor<PD>([&](auto jc) { buf[decltype(jc)::v] = ld(rk, 0u, jc); __builtin_amdgcn_sched_barrier(0); });
  float* scw = sc + wave * (NG * 64) + lane;
  float m = -INFINITY;
  auto kgroup = [&](int g, auto lastc) {
    constexpr bool last = decltype(lastc)::v;
    float x[16];
    const unsigned gb = g * GS;
    sfor<16>([&](auto jc) {
      constexpr int j = decltype(jc)::v;
      const u32x4 kk = buf[j % W];
      float d = dot2(kk[0], qv[0], 0.f);
      d = dot2(kk[1], qv[1], d); float e = dot2(kk[2], qv[2], 0.f); e = dot2(kk[3], qv[3], e);
      x[j] = d + e;
      __builtin_amdgcn_sched_barrier(0);
      if constexpr (j + PD < 16) buf[(j + PD) % W] = ld(rk, gb, IC<j + PD>{});
      else if constexpr (!last) buf[(j + PD) % W] = ld(rk, gb + GS, IC<j + PD - 16>{});
      else buf[(j + PD) % W] = ld(rv, 0u, IC<j + PD - 16>{});
      if constexpr (last && j == 15) sfor<PDV - PD>([&](auto uc) { constexpr int u = PD + decltype(uc)::v; buf[u % W] = ld(rv, 0u, IC<u>{}); });
      __builtin_amdgcn_sched_barrier(0);
    });
#define TR_STEP(HALF, CTRL, BIT) _Pragma("unroll") for (int h = 0; h < (HALF); ++h) { \
      const float lo_ = x[h], hi_ = x[h + (HALF)]; const float t1_ = lo_ + dpp<CTRL>(lo_), t2_ = hi_ + dpp<CTRL>(hi_); \
      x[h] = (c & (BIT)) ? t2_ : t1_; }
    TR_STEP(8, XOR8, 8) TR_STEP(4, XOR7, 4) TR_STEP(2, XOR2, 2) TR_STEP(1, XOR1, 1)
#undef TR_STEP
    const float sv = x[0] * C2;
    m = fmaxf(m, sv);
    scw[g * 64] = sv;
    __builtin_amdgcn_sched_barrier(0);
  };
#pragma unroll 1
  for (int g = 0; g < NG - 1; ++g) kgroup(g, IC<0>{});
  kgroup(NG - 1, IC<1>{});
  m = fmaxf(m, dpp<XOR8>(m)); m = fmaxf(m, dpp<XOR7>(m)); m = fmaxf(m, dpp<XOR2>(m)); m = fmaxf(m, dpp<XOR1>(m));
  m = xrow_max(m);
  float l = 0.f;
  float acc[8];
#pragma unroll
  for (int i = 0; i < 8; ++i) acc[i] = 0.f;
  auto vgroup = [&](int g, auto modec) {
    constexpr int mode = decltype(modec)::v;
    const unsigned gb = g * GS;
    const float pg = __builtin_amdgcn_exp2f(scw[g * 64] - m);
    l += pg;
    sfor<16>([&](auto jc) {
      constexpr int j = decltype(jc)::v;
      const u32x4 vv = buf[j % W];
      const float p = dpp<NEWBCAST + j>(pg);
#pragma unroll
      for (int i = 0; i < 4; ++i) {
        acc[2 * i] = fmaf(p, __uint_as_float(vv[i] << 16), acc[2 * i]);
        acc[2 * i + 1] = fmaf(p, __uint_as_float(vv[i] & 0xffff0000u), acc[2 * i + 1]);
      }
      __builtin_amdgcn_sched_barrier(0);
      constexpr int E = (RAMP > PDV) ? RAMP : PDV;
      constexpr int i0 = mode == 0 ? j + PDV : mode == 1 ? j + PDV + (E - PDV) * j / 16 : j + E;
      constexpr int i1 = mode == 0 ? j + 1 + PDV : mode == 1 ? j + 1 + PDV + (E - PDV) * (j + 1) / 16 : j + 1 + E;
      sfor<i1 - i0>([&](auto uc) {
        constexpr int idx = i0 + decltype(uc)::v;
        if constexpr (idx < 16) buf[idx % W] = ld(rv, gb, IC<idx>{});
        else if constexpr (mode != 2) buf[idx % W] = ld(rv, gb + GS, IC<idx - 16>{});
      });
      __builtin_amdgcn_sched_barrier(0);
    });
  };
#pragma unroll 1
  for (int g = 0; g < NG - 2; ++g) vgroup(g, IC<0>{});
  vgroup(NG - 2, IC<1>{});
  vgroup(NG - 1, IC<2>{});
  l += dpp<XOR8>(l); l += dpp<XOR7>(l); l += dpp<XOR2>(l); l += dpp<XOR1>(l);
  l = xrow_sum(l);
#pragma unroll
  for (int i = 0; i < 8; ++i) acc[i] = xrow_sum(acc[i]);
  float* rec = lds + wave * (D + 4);
  if (lane < 16) {
    *reinterpret_cast<f32x4*>(rec + 4 + 8 * c) = f32x4{acc[0], acc[1], acc[2], acc[3]};
    *reinterpret_cast<f32x4*>(rec + 8 + 8 * c) = f32x4{acc[4], acc[5], acc[6], acc[7]};
    if (lane == 0) *reinterpret_cast<f32x2*>(rec) = f32x2{m, l};
  }
  __syncthreads();
  if (wave == 0) {
    float M = lds[0];
#pragma unroll
    for (int w = 1; w < NW; ++w) M = fmaxf(M, lds[w * (D + 4)]);
    float L = 0.f, o0 = 0.f, o1 = 0.f;
#pragma unroll
    for (int w = 0; w < NW; ++w) {
      const float* r2 = lds + w * (D + 4);
      const f32x2 ml = *reinterpret_cast<const f32x2*>(r2);
      const f32x2 a = *reinterpret_cast<const f32x2*>(r2 + 4 + 2 * lane);
      const float wt = __builtin_amdgcn_exp2f(ml[0] - M);
      L = fmaf(wt, ml[1], L); o0 = fmaf(wt, a[0], o0); o1 = fmaf(wt, a[1], o1);
    }
    const float inv = 1.f / L;
    __hip_bfloat162 o; o.x = __float2bfloat16(o0 * inv); o.y = __float2bfloat16(o1 * inv);
    *reinterpret_cast<__hip_bfloat162*>(out + (size_t)head * D + 2 * lane) = o;
  }
}


extern "C" void kernel_launch(void* const* d_in, const int* in_sizes, int n_in, void* d_out, int out_size, void* d_ws, size_t ws_size,
                              hipStream_t stream) {
  attn3_k<2, 16, 4, 2, 12, 12, 0><<<256, 2 * 64, 0, stream>>>(d_in[0], d_in[1], d_in[2], (__hip_bfloat16*)d_out);
}
```

```cpp
#include <hip/hip_runtime.h>
#include <hip/hip_bf16.h>
#include <stdint.h>

typedef unsigned u32x4 __attribute__((ext_vector_type(4)));
typedef float f32x4 __attribute__((ext_vector_type(4)));
typedef float f32x2 __attribute__((ext_vector_type(2)));
typedef __bf16 bf16x2 __attribute__((ext_vector_type(2)));

constexpr int D = 128, SKV = 4096;
constexpr float C2 = 0.088388347648318440f * 1.4426950408889634f;

__device__ __forceinline__ float dot2(unsigned k, unsigned q, float acc) {
  return __builtin_amdgcn_fdot2_f32_bf16(__builtin_bit_cast(bf16x2, k), __builtin_bit_cast(bf16x2, q), acc, false);
}
template <int CTRL> __device__ __forceinline__ float dpp(float x) {
  return __builtin_bit_cast(float, __builtin_amdgcn_mov_dpp(__builtin_bit_cast(int, x), CTRL, 0xf, 0xf, true));
}
constexpr int XOR1 = 0xB1, XOR2 = 0x4E, XOR7 = 0x141, XOR8 = 0x128, NEWBCAST = 0x150;
__device__ __forceinline__ float xrow_max(float x) {
  auto s = __builtin_amdgcn_permlane16_swap(__float_as_uint(x), __float_as_uint(x), false, false);
  x = fmaxf(__uint_as_float(s[0]), __uint_as_float(s[1]));
  auto t = __builtin_amdgcn_permlane32_swap(__float_as_uint(x), __float_as_uint(x), false, false);
  return fmaxf(__uint_as_float(t[0]), __uint_as_float(t[1]));
}
__device__ __forceinline__ float xrow_sum(float x) {
  auto s = __builtin_amdgcn_permlane16_swap(__float_as_uint(x), __float_as_uint(x), false, false);
  x = __uint_as_float(s[0]) + __uint_as_float(s[1]);
  auto t = __builtin_amdgcn_permlane32_swap(__float_as_uint(x), __float_as_uint(x), false, false);
  return __uint_as_float(t[0]) + __uint_as_float(t[1]);
}
template <int I> struct IC { static constexpr int v = I; };
template <int N, int I = 0, class F> __device__ __forceinline__ void sfor(F&& f) {
  if constexpr (I < N) { f(IC<I>{}); sfor<N, I + 1>(f); }
}

template <int NW, int W, int G, int S0, int PD, int PDV, int E, int TSW, int GL>
__global__ __launch_bounds__(NW * 64, 1) void attn_kernel(const void* __restrict__ q, const void* __restrict__ k, const void* __restrict__ v,
                                                          __hip_bfloat16* __restrict__ out) {
  constexpr int NL = SKV / 4 / NW;
  constexpr int NG = NL / 16;
  constexpr int NT = 19;
  static_assert(16 % W == 0 && 16 % G == 0 && PD <= W && PDV <= W && PD <= PDV && PDV <= E && E <= W && PD <= S0 && S0 <= W, "");
  __shared__ __attribute__((aligned(16))) float lds[NW * (D + 4)];
  __shared__ float sc[SKV];
  const unsigned t0 = (unsigned)__builtin_amdgcn_s_memrealtime();
  const int head = blockIdx.x;
  const int lane = threadIdx.x & 63, wave = __builtin_amdgcn_readfirstlane(threadIdx.x >> 6), c = lane & 15;
  constexpr unsigned HB = SKV * D * 2;
  const size_t hb = (size_t)head * HB;
  const char* kh = (const char*)k + hb;
  const char* vh = (const char*)v + hb;
  auto rsrc = [](const char* p, unsigned n) { return __builtin_amdgcn_make_buffer_rsrc((void*)p, 0, n, 0x00020000); };
  const auto rk = rsrc(kh, HB), rv = rsrc(vh, HB);
  const int voff = lane * 16 + wave * (G * 1024);
  constexpr unsigned GS = 16 * NW * 1024;
  u32x4 buf[W];
  auto ld = [&](auto rs, unsigned gb, auto jc) {
    constexpr int jj = decltype(jc)::v;
    constexpr int off = (jj / G) * NW * G * 1024 + (jj % G) * 1024;
    constexpr int so = off & ~4095, io = off & 4095;
    return __builtin_amdgcn_raw_buffer_load_b128(rs, voff + io, gb + so, NT);
  };
  const u32x4 qv = *reinterpret_cast<const u32x4*>((const char*)q + (size_t)head * (D * 2) + c * 16);
  sfor<S0>([&](auto jc) { buf[decltype(jc)::v] = ld(rk, 0u, jc); __builtin_amdgcn_sched_barrier(0); });
  float* scw = sc + wave * (NG * 64) + lane;
  float m = -INFINITY;
  auto kgroup = [&](int g, auto modec) {
    constexpr int mode = decltype(modec)::v;
    float x[16];
    const unsigned gb = g * GS;
    const bool last = g == NG - 1;
    const auto rn = rsrc(last ? vh : kh, HB);
    const unsigned gn = last ? 0u : gb + GS;
    sfor<16>([&](auto jc) {
      constexpr int j = decltype(jc)::v;
      const u32x4 kk = buf[j % W];
      float d = dot2(kk[0], qv[0], 0.f);
      d = dot2(kk[1], qv[1], d); float e = dot2(kk[2], qv[2], 0.f); e = dot2(kk[3], qv[3], e);
      x[j] = d + e;
      asm volatile("" : "+v"(x[j]));
      __builtin_amdgcn_sched_barrier(0);
      constexpr int i0 = mode == 0 ? j + PD : j + S0 - (S0 - PD) * j / 16;
      constexpr int i1 = mode == 0 ? j + 1 + PD : j + 1 + S0 - (S0 - PD) * (j + 1) / 16;
      if constexpr (i1 > i0) {
        if constexpr (i0 < 16) buf[i0 % W] = ld(rk, gb, IC<i0>{});
        else buf[i0 % W] = ld(rn, gn, IC<i0 - 16>{});
      }
      __builtin_amdgcn_sched_barrier(0);
    });
#define TR_STEP(HALF, CTRL, BIT) _Pragma("unroll") for (int h = 0; h < (HALF); ++h) { \
      const float lo_ = x[h], hi_ = x[h + (HALF)]; const float t1_ = lo_ + dpp<CTRL>(lo_), t2_ = hi_ + dpp<CTRL>(hi_); \
      x[h] = (c & (BIT)) ? t2_ : t1_; }
    TR_STEP(8, XOR8, 8) TR_STEP(4, XOR7, 4) TR_STEP(2, XOR2, 2) TR_STEP(1, XOR1, 1)
#undef TR_STEP
    const float sv = x[0] * C2;
    m = fmaxf(m, sv);
    scw[g * 64] = sv;
    __builtin_amdgcn_sched_barrier(0);
  };
  if constexpr (S0 > PD) {
    kgroup(0, IC<1>{});
#pragma unroll 1
    for (int g = 1; g < NG; ++g) kgroup(g, IC<0>{});
  } else {
#pragma unroll 1
    for (int g = 0; g < NG; ++g) kgroup(g, IC<0>{});
  }
  sfor<PDV - PD>([&](auto uc) { constexpr int u = PD + decltype(uc)::v; buf[u % W] = ld(rv, 0u, IC<u>{}); __builtin_amdgcn_sched_barrier(0); });
  m = fmaxf(m, dpp<XOR8>(m)); m = fmaxf(m, dpp<XOR7>(m)); m = fmaxf(m, dpp<XOR2>(m)); m = fmaxf(m, dpp<XOR1>(m));
  m = xrow_max(m);
  float l = 0.f;
  f32x2 ac[4];
#pragma unroll
  for (int i = 0; i < 4; ++i) ac[i] = f32x2{0.f, 0.f};
  float sn = scw[0];
  auto vgroup = [&](int g, auto modec) {
    constexpr int mode = decltype(modec)::v;
    const unsigned gb = g * GS;
    const bool last = g == NG - 1;
    const auto rn = rsrc(vh, last ? 0u : HB);
    const unsigned gn = last ? 0u : gb + GS;
    const float pg = __builtin_amdgcn_exp2f(sn - m);
    sn = scw[(last ? g : g + 1) * 64];
    l += pg;
    sfor<16>([&](auto jc) {
      constexpr int j = decltype(jc)::v;
      const u32x4 vv = buf[j % W];
      const float p = dpp<NEWBCAST + j>(pg);
      const f32x2 p2 = {p, p};
#pragma unroll
      for (int i = 0; i < 4; ++i)
        ac[i] = __builtin_elementwise_fma(p2, f32x2{__uint_as_float(vv[i] << 16), __uint_as_float(vv[i] & 0xffff0000u)}, ac[i]);
      asm volatile("" : "+v"(ac[0]), "+v"(ac[1]), "+v"(ac[2]), "+v"(ac[3]));
      __builtin_amdgcn_sched_barrier(0);
      constexpr int i0 = mode == 0 ? j + PDV : mode == 1 ? j + PDV + (E - PDV) * j / 16 : j + E;
      constexpr int i1 = mode == 0 ? j + 1 + PDV : mode == 1 ? j + 1 + PDV + (E - PDV) * (j + 1) / 16 : j + 1 + E;
      sfor<i1 - i0>([&](auto uc) {
        constexpr int idx = i0 + decltype(uc)::v;
        if constexpr (idx < 16) buf[idx % W] = ld(rv, gb, IC<idx>{});
        else buf[idx % W] = ld(rn, gn, IC<idx - 16>{});
      });
      __builtin_amdgcn_sched_barrier(0);
    });
  };
  int g = 0;
#pragma unroll 1
  for (;;) {
    const unsigned tn = (unsigned)__builtin_amdgcn_s_memrealtime();
    vgroup(g, IC<0>{});
    ++g;
    if (g >= NG - 2 || (tn - t0 >= (unsigned)TSW && g <= GL)) break;
  }
  vgroup(g, IC<1>{});
#pragma unroll 1
  for (++g; g < NG; ++g) vgroup(g, IC<2>{});
  float acc[8];
#pragma unroll
  for (int i = 0; i < 4; ++i) { acc[2 * i] = ac[i][0]; acc[2 * i + 1] = ac[i][1]; }
  l += dpp<XOR8>(l); l += dpp<XOR7>(l); l += dpp<XOR2>(l); l += dpp<XOR1>(l);
  l = xrow_sum(l);
#pragma unroll
  for (int i = 0; i < 8; ++i) acc[i] = xrow_sum(acc[i]);
  float* rec = lds + wave * (D + 4);
  if (lane < 16) {
    *reinterpret_cast<f32x4*>(rec + 4 + 8 * c) = f32x4{acc[0], acc[1], acc[2], acc[3]};
    *reinterpret_cast<f32x4*>(rec + 8 + 8 * c) = f32x4{acc[4], acc[5], acc[6], acc[7]};
    if (lane == 0) *reinterpret_cast<f32x2*>(rec) = f32x2{m, l};
  }
  __syncthreads();
  if (wave == 0) {
    float M = lds[0];
#pragma unroll
    for (int w = 1; w < NW; ++w) M = fmaxf(M, lds[w * (D + 4)]);
    float L = 0.f, o0 = 0.f, o1 = 0.f;
#pragma unroll
    for (int w = 0; w < NW; ++w) {
      const float* r2 = lds + w * (D + 4);
      const f32x2 ml = *reinterpret_cast<const f32x2*>(r2);
      const f32x2 a = *reinterpret_cast<const f32x2*>(r2 + 4 + 2 * lane);
      const float wt = __builtin_amdgcn_exp2f(ml[0] - M);
      L = fmaf(wt, ml[1], L); o0 = fmaf(wt, a[0], o0); o1 = fmaf(wt, a[1], o1);
    }
    const float inv = 1.f / L;
    __hip_bfloat162 o; o.x = __float2bfloat16(o0 * inv); o.y = __float2bfloat16(o1 * inv);
    *reinterpret_cast<__hip_bfloat162*>(out + (size_t)head * D + 2 * lane) = o;
  }
}

extern "C" void kernel_launch(void* const* d_in, const int* in_sizes, int n_in, void* d_out, int out_size, void* d_ws, size_t ws_size,
                              hipStream_t stream) {
  attn_kernel<2, 16, 4, 11, 11, 11, 16, 6300, 25><<<256, 2 * 64, 0, stream>>>(d_in[0], d_in[1], d_in[2], (__hip_bfloat16*)d_out);
}
```
